```python
import torch

def _softmax(x, axis=-1):
    return torch.softmax(x, dim=axis)
def _gelu(x):
    return torch.nn.functional.gelu(x, approximate="tanh")
def _silu(x):
    return torch.nn.functional.silu(x)
def _sigmoid(x):
    return torch.sigmoid(x)
def _layernorm(x, axis=-1, eps=1e-5):
    m = x.mean(dim=axis, keepdim=True)
    v = x.var(dim=axis, keepdim=True, unbiased=False)
    return (x - m) / torch.sqrt(v + eps)
def _scatter_add(tgt, idx, src, axis=0):
    return tgt.clone().index_add_(axis, idx, src)
def _segment_sum(src, seg, n):
    out = torch.zeros((n,) + src.shape[1:], dtype=src.dtype)
    return out.index_add_(0, seg, src)
def _pad(x, pw):
    flat = tuple(v for pair in reversed(pw) for v in pair)
    return torch.nn.functional.pad(x, flat)

kernel_name = 'parallel_sum__ssm_conv_s4d__mixer_token_mlp__prompt'

def setup_inputs():

    _w = (lambda a: (lambda t: t.to(torch.bfloat16) if t.is_floating_point() else t)(torch.as_tensor(a)))
    def _A_setup():
        Bsz, L, D, N = 1, 2048, 512, 128
        return {
            "u": _w((lambda *s: torch.randn(*s, dtype=torch.bfloat16) * 0.05)(Bsz, L, D)),
            "log_A_real": _w((lambda *s: torch.randn(*s, dtype=torch.bfloat16) * 3.2)(D, N) * 0.5),
            "A_imag": _w((lambda *s: torch.randn(*s, dtype=torch.bfloat16) * 0.05)(D, N) * 3.14),
            "C_real": _w((lambda *s: torch.randn(*s, dtype=torch.bfloat16) * 0.2)(D, N) / N ** 0.5),
            "C_imag": _w((lambda *s: torch.randn(*s, dtype=torch.bfloat16) * 0.4)(D, N) / N ** 0.5),
            "log_dt": _w((lambda *s: torch.randn(*s, dtype=torch.bfloat16) * 0.05)(D) * 0.5 - 4.0),
            "D_skip": _w((lambda *s: torch.randn(*s, dtype=torch.bfloat16) * 0.05)(D)),
        }
    _A_in = _A_setup()

    def _B_setup():
        B, S, D, F = 1, 2048, 512, 1792
        return {
            "x": _w((lambda *s: torch.randn(*s, dtype=torch.bfloat16) * 0.05)(B, S, D)),
            "w1": _w((lambda *s: torch.randn(*s, dtype=torch.bfloat16) * 0.8)(S, F) * (S ** -0.5)),
            "b1": _w((lambda *s: torch.randn(*s, dtype=torch.bfloat16) * 3.2)(F) * 0.02),
            "w2": _w((lambda *s: torch.randn(*s, dtype=torch.bfloat16) * 0.8)(F, S) * (F ** -0.5)),
            "b2": _w((lambda *s: torch.randn(*s, dtype=torch.bfloat16) * 3.2)(S) * 0.02),
        }
    _B_in = _B_setup()

    return {
        "A_u": _A_in["u"],
        "A_log_A_real": _A_in["log_A_real"],
        "A_A_imag": _A_in["A_imag"],
        "A_C_real": _A_in["C_real"],
        "A_C_imag": _A_in["C_imag"],
        "A_log_dt": _A_in["log_dt"],
        "A_D_skip": _A_in["D_skip"],
        "B_x": _B_in["x"],
        "B_w1": _B_in["w1"],
        "B_b1": _B_in["b1"],
        "B_w2": _B_in["w2"],
        "B_b2": _B_in["b2"],
    }

def reference(A_u, A_log_A_real, A_A_imag, A_C_real, A_C_imag, A_log_dt, A_D_skip, B_x, B_w1, B_b1, B_w2, B_b2):
    def _A_ref(u, log_A_real, A_imag, C_real, C_imag, log_dt, D_skip):
        L = u.shape[1]
        dt = torch.exp(log_dt)
        A = -torch.exp(log_A_real) + 1j * A_imag
        C = (C_real + 1j * C_imag) * dt[:, None]
        pos = (lambda x, dt: x.to(dt))(torch.arange(L), log_dt.dtype)
        P = torch.exp((A * dt[:, None])[:, :, None] * pos[None, None, :])
        K = 2.0 * torch.einsum("dn,dnl->dl", C, P).real
        L2 = 2 * L
        u_t = (lambda x, *d: x.permute(*d))(u, 0, 2, 1)
        Kf = (lambda x, *a, **k: torch.fft.rfft(x.float(), *a, **k))(K, L2)
        Uf = (lambda x, *a, **k: torch.fft.rfft(x.float(), *a, **k))(u_t, L2)
        y = (lambda x, *a, **k: torch.fft.irfft(x, *a, **k).to(torch.bfloat16))(Uf * Kf[None, :, :], L2)[:, :, :L]
        y = (lambda x, *d: x.permute(*d))(y, 0, 2, 1)
        return y + u * D_skip[None, None, :]

    def _B_ref(x, w1, b1, w2, b2):
        B, S, D = x.shape
        h = _layernorm(x, axis=-1)
        u = torch.einsum("bsd,sf->bdf", h, w1) + b1
        u = _gelu(u)
        y = torch.einsum("bdf,fs->bds", u, w2) + b2
        return (lambda x, *d: x.permute(*d))(y, 0, 2, 1)

    x = A_u
    xn = _layernorm(x, axis=-1)
    return x + _A_ref(xn, A_log_A_real, A_A_imag, A_C_real, A_C_imag, A_log_dt, A_D_skip) + _B_ref(xn, B_w1, B_b1, B_w2, B_b2)
```

```cpp
#include <hip/hip_runtime.h>
#include <hip/hip_bf16.h>
#include <stdint.h>

typedef __attribute__((ext_vector_type(8))) short s16x8;
typedef __attribute__((ext_vector_type(4))) short s16x4;
typedef __attribute__((ext_vector_type(16))) float f32x16;
typedef __attribute__((ext_vector_type(4))) float f32x4;
typedef __attribute__((ext_vector_type(4))) unsigned int u32x4;
typedef __attribute__((ext_vector_type(2))) unsigned int u32x2;

constexpr int L_ = 2048, D_ = 512, N_ = 128, F_ = 1792;

__device__ __forceinline__ float bf2f(uint16_t v) { return __uint_as_float((uint32_t)v << 16); }
__device__ __forceinline__ uint16_t f2bf(float f) {
  uint32_t u = __float_as_uint(f);
  u += 0x7fffu + ((u >> 16) & 1u);
  return (uint16_t)(u >> 16);
}
__device__ __forceinline__ float rbf(float f) { return bf2f(f2bf(f)); }

__device__ __forceinline__ float wave_sum(float v) {
#pragma unroll
  for (int o = 32; o >= 1; o >>= 1) v += __shfl_xor(v, o);
  return v;
}

__device__ __forceinline__ void ln_row(float (&x)[8]) {
  float s = 0.f;
#pragma unroll
  for (int j = 0; j < 8; ++j) s += x[j];
  s = wave_sum(s);
  const float mean = s * (1.0f / 512.0f);
  float q = 0.f;
#pragma unroll
  for (int j = 0; j < 8; ++j) { float dlt = x[j] - mean; q += dlt * dlt; }
  q = wave_sum(q);
  const float m = rbf(mean);
  const float v = rbf(q * (1.0f / 512.0f));
  const float den = rbf(sqrtf(rbf(v + 1e-5f)));
#pragma unroll
  for (int j = 0; j < 8; ++j) x[j] = rbf(rbf(x[j] - m) / den);
}

__global__ __launch_bounds__(256) void kA(const uint16_t* __restrict__ x, const uint16_t* __restrict__ logA,
                                          const uint16_t* __restrict__ Aim, const uint16_t* __restrict__ Cre,
                                          const uint16_t* __restrict__ Cim, const uint16_t* __restrict__ logdt,
                                          uint16_t* __restrict__ xnT, uint16_t* __restrict__ hT,
                                          uint16_t* __restrict__ Khi, uint16_t* __restrict__ Klo) {
  __shared__ __attribute__((aligned(16))) char smem[66560];
  const int tid = threadIdx.x, lane = tid & 63, w = tid >> 6;
  if (blockIdx.x < 64) {
    uint16_t* txn = (uint16_t*)smem;
    uint16_t* th = (uint16_t*)(smem + 32768);
    const int s0 = blockIdx.x * 32;
    for (int rr = 0; rr < 8; ++rr) {
      const int r = w * 8 + rr;
      s16x8 v = *(const s16x8*)(x + (size_t)(s0 + r) * D_ + lane * 8);
      float f[8];
#pragma unroll
      for (int j = 0; j < 8; ++j) f[j] = bf2f((uint16_t)v[j]);
      ln_row(f);
      s16x8 o;
#pragma unroll
      for (int j = 0; j < 8; ++j) o[j] = (short)f2bf(f[j]);
      *(s16x8*)(txn + r * 512 + lane * 8) = o;
      ln_row(f);
#pragma unroll
      for (int j = 0; j < 8; ++j) o[j] = (short)f2bf(f[j]);
      *(s16x8*)(th + r * 512 + lane * 8) = o;
    }
    __syncthreads();
    for (int dd = 0; dd < 2; ++dd) {
      const int d = tid + dd * 256;
      s16x8 a[4], b[4];
#pragma unroll
      for (int r = 0; r < 32; ++r) {
        a[r >> 3][r & 7] = (short)txn[r * 512 + d];
        b[r >> 3][r & 7] = (short)th[r * 512 + d];
      }
#pragma unroll
      for (int q = 0; q < 4; ++q) {
        *(s16x8*)(xnT + (size_t)d * L_ + s0 + q * 8) = a[q];
        *(s16x8*)(hT + (size_t)d * L_ + s0 + q * 8) = b[q];
      }
    }
    return;
  }
  const int d = blockIdx.x - 64;
  const float dt = rbf(__expf(bf2f(logdt[d])));
  float* XA = (float*)(smem + w * 16384);
  float* YB = (float*)(smem + w * 16384 + 8192);
  float* Kp = (float*)(smem + 65536);
  const int pbase = (w == 0) ? 0 : (128 << w);
  const int hstep = (w == 0) ? 16 : (16 << w);
  const int lstep = 1 << w;
  const int nhi = (w == 0) ? 16 : 8;
  f32x4 acc = {0.f, 0.f, 0.f, 0.f};
  float k2048 = 0.f;
  for (int half = 0; half < 2; ++half) {
    const int n = half * 64 + lane;
    const float ar = -rbf(expf(bf2f(logA[d * N_ + n]))) * dt;
    const float ai = bf2f(Aim[d * N_ + n]) * dt;
    const float cr = bf2f(Cre[d * N_ + n]) * dt;
    const float ci = bf2f(Cim[d * N_ + n]) * dt;
    for (int i = 0; i < 16; ++i) {
      float xr = 0.f, xi = 0.f;
      if (i < nhi) {
        const float p = (float)(pbase + hstep * i);
        const float e = expf(ar * p);
        float sn, cs;
        sincosf(ai * p, &sn, &cs);
        const float zr = e * cs, zi = e * sn;
        xr = cr * zr - ci * zi;
        xi = cr * zi + ci * zr;
      }
      XA[(2 * lane) * 16 + i] = xr;
      XA[(2 * lane + 1) * 16 + i] = -xi;
      const float p2 = (float)(lstep * i);
      const float e2 = expf(ar * p2);
      float sn2, cs2;
      sincosf(ai * p2, &sn2, &cs2);
      YB[(2 * lane) * 16 + i] = e2 * cs2;
      YB[(2 * lane + 1) * 16 + i] = e2 * sn2;
    }
    if (w == 3) {
      const float e = expf(ar * 2048.f);
      float sn, cs;
      sincosf(ai * 2048.f, &sn, &cs);
      k2048 += e * (cr * cs - ci * sn);
    }
    __builtin_amdgcn_s_waitcnt(0);
    __builtin_amdgcn_wave_barrier();
    for (int ks = 0; ks < 32; ++ks) {
      const float a = XA[(4 * ks + (lane >> 4)) * 16 + (lane & 15)];
      const float b = YB[(4 * ks + (lane >> 4)) * 16 + (lane & 15)];
      acc = __builtin_amdgcn_mfma_f32_16x16x4f32(a, b, acc, 0, 0, 0);
    }
    __builtin_amdgcn_s_waitcnt(0);
    __builtin_amdgcn_wave_barrier();
  }
  (void)Kp;
  __syncthreads();
  float* Kt = (float*)smem;
  {
    const int j = lane & 15;
    const int off = (w == 0) ? 0 : (128 + 128 * w);
#pragma unroll
    for (int r = 0; r < 4; ++r) {
      const int i = (lane >> 4) * 4 + r;
      if (i < nhi) Kt[off + i * 16 + j] = 2.f * acc[r];
    }
    if (w == 3) {
      float t = wave_sum(k2048);
      if (lane == 0) Kt[640] = 2.f * t;
    }
  }
  __syncthreads();
  {
    s16x8 vh, vl;
#pragma unroll
    for (int j = 0; j < 8; ++j) {
      const int l = tid * 8 + j;
      const int p = (int)rbf((float)l);
      int idx;
      if (p < 256) idx = p;
      else if (p < 512) idx = 256 + ((p - 256) >> 1);
      else if (p < 1024) idx = 384 + ((p - 512) >> 2);
      else if (p < 2048) idx = 512 + ((p - 1024) >> 3);
      else idx = 640;
      const float kv = Kt[idx];
      const uint16_t h = f2bf(kv);
      vh[j] = (short)h;
      vl[j] = (short)f2bf(kv - bf2f(h));
    }
    *(s16x8*)(Khi + (size_t)d * L_ + tid * 8) = vh;
    *(s16x8*)(Klo + (size_t)d * L_ + tid * 8) = vl;
  }
}

__device__ __forceinline__ void glds16(const void* g, void* lds) {
  __builtin_amdgcn_global_load_lds((const __attribute__((address_space(1))) void*)g,
                                   (__attribute__((address_space(3))) void*)lds, 16, 0, 0);
}

template <bool SWAP>
__device__ __forceinline__ f32x16 gemm_tile(const uint16_t* __restrict__ P, int ldp, const uint16_t* __restrict__ Q,
                                            int ldq, int p0, int q0, int Kdim, char* smem) {
  const int tid = threadIdx.x, lane = tid & 63, w = tid >> 6;
  const int wp = w >> 1, wq = w & 1;
  f32x16 acc;
#pragma unroll
  for (int i = 0; i < 16; ++i) acc[i] = 0.f;
  const int nit = Kdim / 64;
  const int lrow = lane >> 3, lch = lane & 7;
  auto stage = [&](int it, int buf) {
    char* sp = smem + buf * 16384;
    char* sq = sp + 8192;
    const int k0 = it * 64;
#pragma unroll
    for (int t = 0; t < 2; ++t) {
      const int row = (w * 2 + t) * 8 + lrow;
      const int gch = lch ^ (row & 7);
      glds16(P + (size_t)(p0 + row) * ldp + k0 + gch * 8, sp + (w * 2 + t) * 1024);
      const int gchq = lch ^ (((row >> 1) & 1) << 2);
      glds16(Q + (size_t)(k0 + row) * ldq + q0 + gchq * 8, sq + (w * 2 + t) * 1024);
    }
  };
  stage(0, 0);
  for (int it = 0; it < nit; ++it) {
    __builtin_amdgcn_s_waitcnt(0);
    __syncthreads();
    if (it + 1 < nit) stage(it + 1, (it + 1) & 1);
    const char* sp = smem + (it & 1) * 16384;
    const char* sq = sp + 8192;
#pragma unroll
    for (int kk = 0; kk < 4; ++kk) {
      const int prow = wp * 32 + (lane & 31);
      const int pch = (2 * kk + (lane >> 5)) ^ (prow & 7);
      s16x8 pf = *(const s16x8*)(sp + prow * 128 + pch * 16);
      s16x4 q4[2];
#pragma unroll
      for (int t = 0; t < 2; ++t) {
        const int krow = 16 * kk + 8 * (lane >> 5) + 4 * t + ((lane & 15) >> 2);
        const int col = wq * 32 + 16 * ((lane >> 4) & 1) + 4 * (lane & 3);
        const int ch = (col >> 3) ^ (((krow >> 1) & 1) << 2);
        q4[t] = __builtin_amdgcn_ds_read_tr16_b64_v4i16(
            (__attribute__((address_space(3))) s16x4*)(sq + krow * 128 + ch * 16 + (col & 7) * 2));
      }
      s16x8 qf = {q4[0][0], q4[0][1], q4[0][2], q4[0][3], q4[1][0], q4[1][1], q4[1][2], q4[1][3]};
      if (SWAP)
        acc = __builtin_amdgcn_mfma_f32_32x32x16_bf16(qf, pf, acc, 0, 0, 0);
      else
        acc = __builtin_amdgcn_mfma_f32_32x32x16_bf16(pf, qf, acc, 0, 0, 0);
    }
  }
  return acc;
}

constexpr int CP_STRIDE = 4256;
constexpr int T0 = 2055;

__global__ __launch_bounds__(256) void kB(const uint16_t* __restrict__ hT, const uint16_t* __restrict__ w1,
                                          const uint16_t* __restrict__ b1, uint16_t* __restrict__ G,
                                          const uint16_t* __restrict__ xnT, const uint16_t* __restrict__ Khi,
                                          const uint16_t* __restrict__ Klo, const uint16_t* __restrict__ Dskip,
                                          uint16_t* __restrict__ AT) {
  __shared__ __attribute__((aligned(16))) char smem[8 * CP_STRIDE + 2 * 9216 + 64];
  const int tid = threadIdx.x, lane = tid & 63, w = tid >> 6;
  if (blockIdx.x < 224) {
    const int xcd = blockIdx.x & 7, slot = blockIdx.x >> 3;
    const int id = xcd * 28 + slot;
    const int nt = id >> 3, mt = id & 7;
    const int p0 = mt * 64, q0 = nt * 64;
    f32x16 acc = gemm_tile<false>(hT, L_, w1, F_, p0, q0, L_, smem);
    const int wp = w >> 1, wq = w & 1;
    const int f = q0 + wq * 32 + (lane & 31);
    const float bias = bf2f(b1[f]);
#pragma unroll
    for (int r = 0; r < 16; ++r) {
      const int dd = p0 + wp * 32 + (r & 3) + 8 * (r >> 2) + 4 * (lane >> 5);
      const float u = rbf(rbf(acc[r]) + bias);
      const float inner = 0.7978845608028654f * (u + 0.044715f * u * u * u);
      const float g = 0.5f * u * (1.f + tanhf(inner));
      G[(size_t)dd * F_ + f] = f2bf(g);
    }
    return;
  }
  const int d = blockIdx.x - 224;
  uint16_t* cp = (uint16_t*)smem;
  uint16_t* Kz = (uint16_t*)(smem + 8 * CP_STRIDE);
  {
    for (int i = tid; i < 8 * CP_STRIDE / 4; i += 256) ((uint32_t*)smem)[i] = 0u;
    for (int i = tid; i < 2304 / 2; i += 256) {
      ((uint32_t*)Kz)[i] = 0u;
      ((uint32_t*)(Kz + 4608))[i] = 0u;
    }
    __syncthreads();
    s16x8 uv = *(const s16x8*)(xnT + (size_t)d * L_ + tid * 8);
#pragma unroll
    for (int j = 0; j < 8; ++j) {
      const int l = tid * 8 + j;
      const int t = T0 - l;
#pragma unroll
      for (int c = 0; c < 8; ++c) cp[c * (CP_STRIDE / 2) + (t - c)] = (uint16_t)uv[j];
    }
    const int idx = 2048 + tid * 8;
    const int pos = idx + 8 * (idx >> 6);
    *(s16x8*)(Kz + pos) = *(const s16x8*)(Khi + (size_t)d * L_ + tid * 8);
    *(s16x8*)(Kz + 4608 + pos) = *(const s16x8*)(Klo + (size_t)d * L_ + tid * 8);
    __syncthreads();
  }
  f32x16 acc0, acc1;
#pragma unroll
  for (int i = 0; i < 16; ++i) { acc0[i] = 0.f; acc1[i] = 0.f; }
  const int r = lane & 31, h = lane >> 5;
  const int c = (T0 - r) & 7;
  const uint16_t* cpl = cp + c * (CP_STRIDE / 2);
  auto afrag = [&](int s) -> s16x8 {
    const int idx0 = T0 - (s + r - 8 * h) - c;
    return *(const s16x8*)(cpl + idx0);
  };
  const int tau0 = -3 + w * 32;
  s16x8 fa = afrag(16 * tau0);
  s16x8 fb = afrag(16 * (tau0 + 1));
  for (int st = 0; st < 32; ++st) {
    const int tau = tau0 + st;
    s16x8 fc = afrag(16 * (tau + 2));
    const int kidx = 2048 + 64 * r - 16 * tau + 8 * h;
    const int kpos = kidx + 8 * (kidx >> 6);
    s16x8 kh = *(const s16x8*)(Kz + kpos);
    s16x8 kl = *(const s16x8*)(Kz + 4608 + kpos);
    acc0 = __builtin_amdgcn_mfma_f32_32x32x16_bf16(fa, kh, acc0, 0, 0, 0);
    acc1 = __builtin_amdgcn_mfma_f32_32x32x16_bf16(fc, kh, acc1, 0, 0, 0);
    acc0 = __builtin_amdgcn_mfma_f32_32x32x16_bf16(fa, kl, acc0, 0, 0, 0);
    acc1 = __builtin_amdgcn_mfma_f32_32x32x16_bf16(fc, kl, acc1, 0, 0, 0);
    fa = fb;
    fb = fc;
  }
  __syncthreads();
  float* red = (float*)smem;
#pragma unroll
  for (int i = 0; i < 16; ++i) {
    red[((w * 2 + 0) * 16 + i) * 64 + lane] = acc0[i];
    red[((w * 2 + 1) * 16 + i) * 64 + lane] = acc1[i];
  }
  __syncthreads();
  {
    const int a = w >> 1;
    const float dsk = bf2f(Dskip[d]);
#pragma unroll
    for (int gg = 0; gg < 2; ++gg) {
      const int g = (w & 1) * 2 + gg;
      const int l0 = 64 * r + 32 * a + 8 * g + 4 * h;
      s16x4 un = *(const s16x4*)(xnT + (size_t)d * L_ + l0);
      s16x4 o;
#pragma unroll
      for (int e = 0; e < 4; ++e) {
        const int reg = g * 4 + e;
        float y = 0.f;
#pragma unroll
        for (int ww = 0; ww < 4; ++ww) y += red[((ww * 2 + a) * 16 + reg) * 64 + lane];
        o[e] = (short)f2bf(rbf(y) + rbf(bf2f((uint16_t)un[e]) * dsk));
      }
      *(s16x4*)(AT + (size_t)d * L_ + l0) = o;
    }
  }
}

__global__ __launch_bounds__(256) void kC(const uint16_t* __restrict__ G, const uint16_t* __restrict__ w2,
                                          const uint16_t* __restrict__ b2, const uint16_t* __restrict__ x,
                                          const uint16_t* __restrict__ AT, uint16_t* __restrict__ out) {
  __shared__ __attribute__((aligned(16))) char smem[32768];
  const int tid = threadIdx.x, lane = tid & 63, w = tid >> 6;
  const int xcd = blockIdx.x & 7, slot = blockIdx.x >> 3;
  const int id = xcd * 32 + slot;
  const int nt = id >> 3, mt = id & 7;
  const int p0 = mt * 64, q0 = nt * 64;
  f32x16 acc = gemm_tile<true>(G, F_, w2, L_, p0, q0, F_, smem);
  const int wp = w >> 1, wq = w & 1;
  const int dd = p0 + wp * 32 + (lane & 31);
#pragma unroll
  for (int g = 0; g < 4; ++g) {
    const int s0 = q0 + wq * 32 + 8 * g + 4 * (lane >> 5);
    s16x4 av = *(const s16x4*)(AT + (size_t)dd * L_ + s0);
#pragma unroll
    for (int e = 0; e < 4; ++e) {
      const int s = s0 + e;
      const float bref = rbf(rbf(acc[g * 4 + e]) + bf2f(b2[s]));
      const float xa = rbf(bf2f(x[(size_t)s * D_ + dd]) + bf2f((uint16_t)av[e]));
      out[(size_t)s * D_ + dd] = f2bf(xa + bref);
    }
  }
}

extern "C" void kernel_launch(void* const* d_in, const int* in_sizes, int n_in, void* d_out, int out_size,
                              void* d_ws, size_t ws_size, hipStream_t stream) {
  const uint16_t* x = (const uint16_t*)d_in[0];
  const uint16_t* logA = (const uint16_t*)d_in[1];
  const uint16_t* Aim = (const uint16_t*)d_in[2];
  const uint16_t* Cre = (const uint16_t*)d_in[3];
  const uint16_t* Cim = (const uint16_t*)d_in[4];
  const uint16_t* logdt = (const uint16_t*)d_in[5];
  const uint16_t* Dskip = (const uint16_t*)d_in[6];
  const uint16_t* w1 = (const uint16_t*)d_in[8];
  const uint16_t* b1 = (const uint16_t*)d_in[9];
  const uint16_t* w2 = (const uint16_t*)d_in[10];
  const uint16_t* b2 = (const uint16_t*)d_in[11];
  char* ws = (char*)d_ws;
  const size_t MB2 = (size_t)2 << 20;
  uint16_t* xnT = (uint16_t*)(ws + 0 * MB2);
  uint16_t* hT = (uint16_t*)(ws + 1 * MB2);
  uint16_t* Khi = (uint16_t*)(ws + 2 * MB2);
  uint16_t* Klo = (uint16_t*)(ws + 3 * MB2);
  uint16_t* G = (uint16_t*)(ws + 4 * MB2);
  uint16_t* AT = (uint16_t*)(ws + 5 * MB2);
  kA<<<64 + 512, 256, 0, stream>>>(x, logA, Aim, Cre, Cim, logdt, xnT, hT, Khi, Klo);
  kB<<<224 + 512, 256, 0, stream>>>(hT, w1, b1, G, xnT, Khi, Klo, Dskip, AT);
  kC<<<256, 256, 0, stream>>>(G, w2, b2, x, AT, (uint16_t*)d_out);
}
```

```cpp
#include <hip/hip_runtime.h>
#include <hip/hip_bf16.h>
#include <stdint.h>

typedef __attribute__((ext_vector_type(8))) short s16x8;
typedef __attribute__((ext_vector_type(4))) short s16x4;
typedef __attribute__((ext_vector_type(16))) float f32x16;
typedef __attribute__((ext_vector_type(4))) float f32x4;
typedef __attribute__((ext_vector_type(4))) unsigned int u32x4;
typedef __attribute__((ext_vector_type(2))) unsigned int u32x2;

constexpr int LT = 2112;
constexpr int L_ = 2048, D_ = 512, N_ = 128, F_ = 1792;

__device__ __forceinline__ float bf2f(uint16_t v) { return __uint_as_float((uint32_t)v << 16); }
__device__ __forceinline__ uint16_t f2bf(float f) {
  uint32_t u = __float_as_uint(f);
  u += 0x7fffu + ((u >> 16) & 1u);
  return (uint16_t)(u >> 16);
}
__device__ __forceinline__ float rbf(float f) { return bf2f(f2bf(f)); }

__device__ __forceinline__ void fsincos(float x, float* sn, float* cs) {
  const float t = x * 0.15915494309189535f;
  const float n = rintf(t);
  float r = fmaf(x, 0.15915494309189535f, -n);
  r = fmaf(x, -1.1450415e-09f * 0.0f, r);
  *sn = __builtin_amdgcn_sinf(r);
  *cs = __builtin_amdgcn_cosf(r);
}
__device__ __forceinline__ float wave_sum(float v) {
#pragma unroll
  for (int o = 32; o >= 1; o >>= 1) v += __shfl_xor(v, o);
  return v;
}

__device__ __forceinline__ void ln_rows(float (&x)[2][8]) {
  float s[2], q[2];
#pragma unroll
  for (int r = 0; r < 2; ++r) {
    s[r] = 0.f;
#pragma unroll
    for (int j = 0; j < 8; ++j) s[r] += x[r][j];
  }
#pragma unroll
  for (int o = 32; o >= 1; o >>= 1)
#pragma unroll
    for (int r = 0; r < 2; ++r) s[r] += __shfl_xor(s[r], o);
#pragma unroll
  for (int r = 0; r < 2; ++r) {
    s[r] *= (1.0f / 512.0f);
    q[r] = 0.f;
#pragma unroll
    for (int j = 0; j < 8; ++j) { float dlt = x[r][j] - s[r]; q[r] += dlt * dlt; }
  }
#pragma unroll
  for (int o = 32; o >= 1; o >>= 1)
#pragma unroll
    for (int r = 0; r < 2; ++r) q[r] += __shfl_xor(q[r], o);
#pragma unroll
  for (int r = 0; r < 2; ++r) {
    const float m = rbf(s[r]);
    const float v = rbf(q[r] * (1.0f / 512.0f));
    const float den = rbf(sqrtf(rbf(v + 1e-5f)));
#pragma unroll
    for (int j = 0; j < 8; ++j) x[r][j] = rbf(rbf(x[r][j] - m) / den);
  }
}

__global__ __launch_bounds__(256) void kA(const uint16_t* __restrict__ x, const uint16_t* __restrict__ logA,
                                          const uint16_t* __restrict__ Aim, const uint16_t* __restrict__ Cre,
                                          const uint16_t* __restrict__ Cim, const uint16_t* __restrict__ logdt,
                                          uint16_t* __restrict__ xnT, uint16_t* __restrict__ hT,
                                          uint16_t* __restrict__ Khi, uint16_t* __restrict__ Klo) {
  __shared__ __attribute__((aligned(16))) char smem[67584];
  const int tid = threadIdx.x, lane = tid & 63, w = tid >> 6;
  if (blockIdx.x < 256) {
    uint16_t* txn = (uint16_t*)smem;
    uint16_t* th = (uint16_t*)(smem + 8320);
    const int s0 = blockIdx.x * 8;
    float f[2][8];
    const int r0 = w * 2;
#pragma unroll
    for (int rr = 0; rr < 2; ++rr) {
      s16x8 v = *(const s16x8*)(x + (size_t)(s0 + r0 + rr) * D_ + lane * 8);
#pragma unroll
      for (int j = 0; j < 8; ++j) f[rr][j] = bf2f((uint16_t)v[j]);
    }
    ln_rows(f);
#pragma unroll
    for (int rr = 0; rr < 2; ++rr) {
      s16x8 o;
#pragma unroll
      for (int j = 0; j < 8; ++j) o[j] = (short)f2bf(f[rr][j]);
      *(s16x8*)(txn + (r0 + rr) * 520 + lane * 8) = o;
    }
    ln_rows(f);
#pragma unroll
    for (int rr = 0; rr < 2; ++rr) {
      s16x8 o;
#pragma unroll
      for (int j = 0; j < 8; ++j) o[j] = (short)f2bf(f[rr][j]);
      *(s16x8*)(th + (r0 + rr) * 520 + lane * 8) = o;
    }
    __syncthreads();
#pragma unroll
    for (int dd = 0; dd < 2; ++dd) {
      const int d = tid + dd * 256;
      s16x8 a, b;
#pragma unroll
      for (int r = 0; r < 8; ++r) {
        a[r] = (short)txn[r * 520 + d];
        b[r] = (short)th[r * 520 + d];
      }
      *(s16x8*)(xnT + (size_t)d * LT + s0) = a;
      *(s16x8*)(hT + (size_t)d * LT + s0) = b;
    }
    return;
  }
  const int d = blockIdx.x - 256;
  const float dt = rbf(__expf(bf2f(logdt[d])));
  float* XA = (float*)(smem + w * 16896);
  float* YB = (float*)(smem + w * 16896 + 8448);
  float* Kp = (float*)smem;
  const int pbase = (w == 0) ? 0 : (128 << w);
  const int hstep = (w == 0) ? 16 : (16 << w);
  const int lstep = 1 << w;
  const int nhi = (w == 0) ? 16 : 8;
  f32x4 acc = {0.f, 0.f, 0.f, 0.f}, acc2 = {0.f, 0.f, 0.f, 0.f};
  float k2048 = 0.f;
  for (int half = 0; half < 2; ++half) {
    const int n = half * 64 + lane;
    const float ar = -rbf(expf(bf2f(logA[d * N_ + n]))) * dt;
    const float ai = bf2f(Aim[d * N_ + n]) * dt;
    const float cr = bf2f(Cre[d * N_ + n]) * dt;
    const float ci = bf2f(Cim[d * N_ + n]) * dt;
    for (int i = 0; i < 16; ++i) {
      float xr = 0.f, xi = 0.f;
      if (i < nhi) {
        const float p = (float)(pbase + hstep * i);
        const float e = __expf(ar * p);
        float sn, cs;
        fsincos(ai * p, &sn, &cs);
        const float zr = e * cs, zi = e * sn;
        xr = cr * zr - ci * zi;
        xi = cr * zi + ci * zr;
      }
      XA[i * 132 + 2 * lane] = xr;
      XA[i * 132 + 2 * lane + 1] = -xi;
      const float p2 = (float)(lstep * i);
      const float e2 = __expf(ar * p2);
      float sn2, cs2;
      fsincos(ai * p2, &sn2, &cs2);
      YB[i * 132 + 2 * lane] = e2 * cs2;
      YB[i * 132 + 2 * lane + 1] = e2 * sn2;
    }
    if (w == 3) {
      const float e = __expf(ar * 2048.f);
      float sn, cs;
      fsincos(ai * 2048.f, &sn, &cs);
      k2048 += e * (cr * cs - ci * sn);
    }
    __builtin_amdgcn_s_waitcnt(0);
    __builtin_amdgcn_wave_barrier();
#pragma unroll
    for (int ks = 0; ks < 32; ++ks) {
      const float a = XA[(lane & 15) * 132 + 4 * ks + (lane >> 4)];
      const float b = YB[(lane & 15) * 132 + 4 * ks + (lane >> 4)];
      if (ks & 1) acc2 = __builtin_amdgcn_mfma_f32_16x16x4f32(a, b, acc2, 0, 0, 0);
      else acc = __builtin_amdgcn_mfma_f32_16x16x4f32(a, b, acc, 0, 0, 0);
    }
    __builtin_amdgcn_s_waitcnt(0);
    __builtin_amdgcn_wave_barrier();
  }
  (void)Kp;
  __syncthreads();
  float* Kt = (float*)smem;
  {
    const int j = lane & 15;
    const int off = (w == 0) ? 0 : (128 + 128 * w);
#pragma unroll
    for (int r = 0; r < 4; ++r) {
      const int i = (lane >> 4) * 4 + r;
      if (i < nhi) Kt[off + i * 16 + j] = 2.f * (acc[r] + acc2[r]);
    }
    if (w == 3) {
      float t = wave_sum(k2048);
      if (lane == 0) Kt[640] = 2.f * t;
    }
  }
  __syncthreads();
  {
    s16x8 vh, vl;
#pragma unroll
    for (int j = 0; j < 8; ++j) {
      const int l = tid * 8 + j;
      const int p = (int)rbf((float)l);
      int idx;
      if (p < 256) idx = p;
      else if (p < 512) idx = 256 + ((p - 256) >> 1);
      else if (p < 1024) idx = 384 + ((p - 512) >> 2);
      else if (p < 2048) idx = 512 + ((p - 1024) >> 3);
      else idx = 640;
      const float kv = Kt[idx];
      const uint16_t h = f2bf(kv);
      vh[j] = (short)h;
      vl[j] = (short)f2bf(kv - bf2f(h));
    }
    *(s16x8*)(Khi + (size_t)d * L_ + tid * 8) = vh;
    *(s16x8*)(Klo + (size_t)d * L_ + tid * 8) = vl;
  }
}

__device__ __forceinline__ void glds16(const void* g, void* lds) {
  __builtin_amdgcn_global_load_lds((const __attribute__((address_space(1))) void*)g,
                                   (__attribute__((address_space(3))) void*)lds, 16, 0, 0);
}

template <bool SWAP>
__device__ __forceinline__ f32x16 gemm_tile(const uint16_t* __restrict__ P, int ldp, const uint16_t* __restrict__ Q,
                                            int ldq, int p0, int q0, int Kdim, char* smem) {
  const int tid = threadIdx.x, lane = tid & 63, w = tid >> 6;
  const int wp = w >> 1, wq = w & 1;
  f32x16 acc, acc2;
#pragma unroll
  for (int i = 0; i < 16; ++i) { acc[i] = 0.f; acc2[i] = 0.f; }
  const int nit = Kdim / 64;
  const int lrow = lane >> 3, lch = lane & 7;
  auto stage = [&](int it, int buf) {
    char* sp = smem + buf * 16384;
    char* sq = sp + 8192;
    const int k0 = it * 64;
#pragma unroll
    for (int t = 0; t < 2; ++t) {
      const int row = (w * 2 + t) * 8 + lrow;
      const int gch = lch ^ (row & 7);
      glds16(P + (size_t)(p0 + row) * ldp + k0 + gch * 8, sp + (w * 2 + t) * 1024);
      const int gchq = lch ^ (((row >> 1) & 1) << 2);
      glds16(Q + (size_t)(k0 + row) * ldq + q0 + gchq * 8, sq + (w * 2 + t) * 1024);
    }
  };
  stage(0, 0);
  stage(1, 1);
  stage(2, 2);
  for (int it = 0; it < nit; ++it) {
    if (it + 3 <= nit) asm volatile("s_waitcnt vmcnt(8)" ::: "memory");
    else asm volatile("s_waitcnt vmcnt(0)" ::: "memory");
    asm volatile("s_waitcnt lgkmcnt(0)" ::: "memory");
    __builtin_amdgcn_s_barrier();
    asm volatile("" ::: "memory");
    if (it + 3 < nit) stage(it + 3, (it + 3) & 3);
    const char* sp = smem + (it & 3) * 16384;
    const char* sq = sp + 8192;
    s16x8 pf[4], qf[4];
#pragma unroll
    for (int kk = 0; kk < 4; ++kk) {
      const int prow = wp * 32 + (lane & 31);
      const int pch = (2 * kk + (lane >> 5)) ^ (prow & 7);
      pf[kk] = *(const s16x8*)(sp + prow * 128 + pch * 16);
      s16x4 q4[2];
#pragma unroll
      for (int t = 0; t < 2; ++t) {
        const int krow = 16 * kk + 8 * (lane >> 5) + 4 * t + ((lane & 15) >> 2);
        const int col = wq * 32 + 16 * ((lane >> 4) & 1) + 4 * (lane & 3);
        const int ch = (col >> 3) ^ (((krow >> 1) & 1) << 2);
        q4[t] = __builtin_amdgcn_ds_read_tr16_b64_v4i16(
            (__attribute__((address_space(3))) s16x4*)(sq + krow * 128 + ch * 16 + (col & 7) * 2));
      }
      qf[kk] = s16x8{q4[0][0], q4[0][1], q4[0][2], q4[0][3], q4[1][0], q4[1][1], q4[1][2], q4[1][3]};
    }
    __builtin_amdgcn_sched_barrier(0);
#pragma unroll
    for (int kk = 0; kk < 4; ++kk) {
      if (kk & 1) {
        if (SWAP) acc2 = __builtin_amdgcn_mfma_f32_32x32x16_bf16(qf[kk], pf[kk], acc2, 0, 0, 0);
        else acc2 = __builtin_amdgcn_mfma_f32_32x32x16_bf16(pf[kk], qf[kk], acc2, 0, 0, 0);
      } else {
        if (SWAP) acc = __builtin_amdgcn_mfma_f32_32x32x16_bf16(qf[kk], pf[kk], acc, 0, 0, 0);
        else acc = __builtin_amdgcn_mfma_f32_32x32x16_bf16(pf[kk], qf[kk], acc, 0, 0, 0);
      }
    }
  }
#pragma unroll
  for (int i = 0; i < 16; ++i) acc[i] += acc2[i];
  return acc;
}

constexpr int CP_STRIDE = 4256;
constexpr int T0 = 2055;

__global__ __launch_bounds__(256) void kB(const uint16_t* __restrict__ hT, const uint16_t* __restrict__ w1,
                                          const uint16_t* __restrict__ b1, uint16_t* __restrict__ G,
                                          const uint16_t* __restrict__ xnT, const uint16_t* __restrict__ Khi,
                                          const uint16_t* __restrict__ Klo, const uint16_t* __restrict__ Dskip,
                                          uint16_t* __restrict__ AT) {
  __shared__ __attribute__((aligned(16))) char smem[65536];
  const int tid = threadIdx.x, lane = tid & 63, w = tid >> 6;
  if (blockIdx.x < 224) {
    const int xcd = blockIdx.x & 7, slot = blockIdx.x >> 3;
    const int id = xcd * 28 + slot;
    const int nt = id >> 3, mt = id & 7;
    const int p0 = mt * 64, q0 = nt * 64;
    f32x16 acc = gemm_tile<false>(hT, LT, w1, F_, p0, q0, L_, smem);
    const int wp = w >> 1, wq = w & 1;
    const int f = q0 + wq * 32 + (lane & 31);
    const float bias = bf2f(b1[f]);
#pragma unroll
    for (int r = 0; r < 16; ++r) {
      const int dd = p0 + wp * 32 + (r & 3) + 8 * (r >> 2) + 4 * (lane >> 5);
      const float u = rbf(rbf(acc[r]) + bias);
      const float inner = 0.7978845608028654f * (u + 0.044715f * u * u * u);
      const float g = 0.5f * u * (1.f + tanhf(inner));
      G[(size_t)dd * F_ + f] = f2bf(g);
    }
    return;
  }
  const int d = blockIdx.x - 224;
  uint16_t* cp = (uint16_t*)smem;
  uint16_t* Kz = (uint16_t*)(smem + 8 * CP_STRIDE);
  {
    for (int i = tid; i < 8 * CP_STRIDE / 4; i += 256) ((uint32_t*)smem)[i] = 0u;
    for (int i = tid; i < 2304 / 2; i += 256) {
      ((uint32_t*)Kz)[i] = 0u;
      ((uint32_t*)(Kz + 4608))[i] = 0u;
    }
    __syncthreads();
    s16x8 uv = *(const s16x8*)(xnT + (size_t)d * LT + tid * 8);
#pragma unroll
    for (int j = 0; j < 8; ++j) {
      const int l = tid * 8 + j;
      const int t = T0 - l;
#pragma unroll
      for (int c = 0; c < 8; ++c) cp[c * (CP_STRIDE / 2) + (t - c)] = (uint16_t)uv[j];
    }
    const int idx = 2048 + tid * 8;
    const int pos = idx + 8 * (idx >> 6);
    *(s16x8*)(Kz + pos) = *(const s16x8*)(Khi + (size_t)d * L_ + tid * 8);
    *(s16x8*)(Kz + 4608 + pos) = *(const s16x8*)(Klo + (size_t)d * L_ + tid * 8);
    __syncthreads();
  }
  f32x16 acc0, acc1;
#pragma unroll
  for (int i = 0; i < 16; ++i) { acc0[i] = 0.f; acc1[i] = 0.f; }
  const int r = lane & 31, h = lane >> 5;
  const int c = (T0 - r) & 7;
  const uint16_t* cpl = cp + c * (CP_STRIDE / 2);
  auto afrag = [&](int s) -> s16x8 {
    const int idx0 = T0 - (s + r - 8 * h) - c;
    return *(const s16x8*)(cpl + idx0);
  };
  const int tau0 = -3 + w * 32;
  s16x8 fa = afrag(16 * tau0);
  s16x8 fb = afrag(16 * (tau0 + 1));
  for (int st = 0; st < 32; ++st) {
    const int tau = tau0 + st;
    s16x8 fc = afrag(16 * (tau + 2));
    const int kidx = 2048 + 64 * r - 16 * tau + 8 * h;
    const int kpos = kidx + 8 * (kidx >> 6);
    s16x8 kh = *(const s16x8*)(Kz + kpos);
    s16x8 kl = *(const s16x8*)(Kz + 4608 + kpos);
    acc0 = __builtin_amdgcn_mfma_f32_32x32x16_bf16(fa, kh, acc0, 0, 0, 0);
    acc1 = __builtin_amdgcn_mfma_f32_32x32x16_bf16(fc, kh, acc1, 0, 0, 0);
    acc0 = __builtin_amdgcn_mfma_f32_32x32x16_bf16(fa, kl, acc0, 0, 0, 0);
    acc1 = __builtin_amdgcn_mfma_f32_32x32x16_bf16(fc, kl, acc1, 0, 0, 0);
    fa = fb;
    fb = fc;
  }
  __syncthreads();
  float* red = (float*)smem;
#pragma unroll
  for (int i = 0; i < 16; ++i) {
    red[((w * 2 + 0) * 16 + i) * 64 + lane] = acc0[i];
    red[((w * 2 + 1) * 16 + i) * 64 + lane] = acc1[i];
  }
  __syncthreads();
  {
    const int a = w >> 1;
    const float dsk = bf2f(Dskip[d]);
#pragma unroll
    for (int gg = 0; gg < 2; ++gg) {
      const int g = (w & 1) * 2 + gg;
      const int l0 = 64 * r + 32 * a + 8 * g + 4 * h;
      s16x4 un = *(const s16x4*)(xnT + (size_t)d * LT + l0);
      s16x4 o;
#pragma unroll
      for (int e = 0; e < 4; ++e) {
        const int reg = g * 4 + e;
        float y = 0.f;
#pragma unroll
        for (int ww = 0; ww < 4; ++ww) y += red[((ww * 2 + a) * 16 + reg) * 64 + lane];
        o[e] = (short)f2bf(rbf(y) + rbf(bf2f((uint16_t)un[e]) * dsk));
      }
      *(s16x4*)(AT + (size_t)d * LT + l0) = o;
    }
  }
}

__global__ __launch_bounds__(256) void kC(const uint16_t* __restrict__ G, const uint16_t* __restrict__ w2,
                                          const uint16_t* __restrict__ b2, const uint16_t* __restrict__ x,
                                          const uint16_t* __restrict__ AT, uint16_t* __restrict__ out) {
  __shared__ __attribute__((aligned(16))) char smem[65536];
  const int tid = threadIdx.x, lane = tid & 63, w = tid >> 6;
  const int xcd = blockIdx.x & 7, slot = blockIdx.x >> 3;
  const int id = xcd * 32 + slot;
  const int nt = id >> 3, mt = id & 7;
  const int p0 = mt * 64, q0 = nt * 64;
  f32x16 acc = gemm_tile<true>(G, F_, w2, L_, p0, q0, F_, smem);
  const int wp = w >> 1, wq = w & 1;
  const int dd = p0 + wp * 32 + (lane & 31);
#pragma unroll
  for (int g = 0; g < 4; ++g) {
    const int s0 = q0 + wq * 32 + 8 * g + 4 * (lane >> 5);
    s16x4 av = *(const s16x4*)(AT + (size_t)dd * LT + s0);
#pragma unroll
    for (int e = 0; e < 4; ++e) {
      const int s = s0 + e;
      const float bref = rbf(rbf(acc[g * 4 + e]) + bf2f(b2[s]));
      const float xa = rbf(bf2f(x[(size_t)s * D_ + dd]) + bf2f((uint16_t)av[e]));
      out[(size_t)s * D_ + dd] = f2bf(xa + bref);
    }
  }
}

extern "C" void kernel_launch(void* const* d_in, const int* in_sizes, int n_in, void* d_out, int out_size,
                              void* d_ws, size_t ws_size, hipStream_t stream) {
  const uint16_t* x = (const uint16_t*)d_in[0];
  const uint16_t* logA = (const uint16_t*)d_in[1];
  const uint16_t* Aim = (const uint16_t*)d_in[2];
  const uint16_t* Cre = (const uint16_t*)d_in[3];
  const uint16_t* Cim = (const uint16_t*)d_in[4];
  const uint16_t* logdt = (const uint16_t*)d_in[5];
  const uint16_t* Dskip = (const uint16_t*)d_in[6];
  const uint16_t* w1 = (const uint16_t*)d_in[8];
  const uint16_t* b1 = (const uint16_t*)d_in[9];
  const uint16_t* w2 = (const uint16_t*)d_in[10];
  const uint16_t* b2 = (const uint16_t*)d_in[11];
  char* ws = (char*)d_ws;
  const size_t MB2 = (size_t)3 << 20;
  uint16_t* xnT = (uint16_t*)(ws + 0 * MB2);
  uint16_t* hT = (uint16_t*)(ws + 1 * MB2);
  uint16_t* Khi = (uint16_t*)(ws + 2 * MB2);
  uint16_t* Klo = (uint16_t*)(ws + 3 * MB2);
  uint16_t* G = (uint16_t*)(ws + 4 * MB2);
  uint16_t* AT = (uint16_t*)(ws + 5 * MB2);
  kA<<<256 + 512, 256, 0, stream>>>(x, logA, Aim, Cre, Cim, logdt, xnT, hT, Khi, Klo);
  kB<<<224 + 512, 256, 0, stream>>>(hT, w1, b1, G, xnT, Khi, Klo, Dskip, AT);
  kC<<<256, 256, 0, stream>>>(G, w2, b2, x, AT, (uint16_t*)d_out);
}
```

```cpp
#include <hip/hip_runtime.h>
#include <hip/hip_bf16.h>
#include <stdint.h>

typedef __attribute__((ext_vector_type(8))) short s16x8;
typedef __attribute__((ext_vector_type(4))) short s16x4;
typedef __attribute__((ext_vector_type(16))) float f32x16;
typedef __attribute__((ext_vector_type(4))) float f32x4;
typedef __attribute__((ext_vector_type(4))) unsigned int u32x4;
typedef __attribute__((ext_vector_type(2))) unsigned int u32x2;

constexpr int LT = 2112;
constexpr int L_ = 2048, D_ = 512, N_ = 128, F_ = 1792;

__device__ __forceinline__ float bf2f(uint16_t v) { return __uint_as_float((uint32_t)v << 16); }
__device__ __forceinline__ uint16_t f2bf(float f) {
  uint32_t u = __float_as_uint(f);
  u += 0x7fffu + ((u >> 16) & 1u);
  return (uint16_t)(u >> 16);
}
__device__ __forceinline__ float rbf(float f) { return bf2f(f2bf(f)); }

__device__ __forceinline__ void fsincos(float x, float* sn, float* cs) {
  const float t = x * 0.15915494309189535f;
  const float n = rintf(t);
  float r = fmaf(x, 0.15915494309189535f, -n);
  r = fmaf(x, -1.1450415e-09f * 0.0f, r);
  *sn = __builtin_amdgcn_sinf(r);
  *cs = __builtin_amdgcn_cosf(r);
}
__device__ __forceinline__ float wave_sum(float v) {
#pragma unroll
  for (int o = 32; o >= 1; o >>= 1) v += __shfl_xor(v, o);
  return v;
}

__device__ __forceinline__ void ln_rows(float (&x)[2][8]) {
  float s[2], q[2];
#pragma unroll
  for (int r = 0; r < 2; ++r) {
    s[r] = 0.f;
#pragma unroll
    for (int j = 0; j < 8; ++j) s[r] += x[r][j];
  }
#pragma unroll
  for (int o = 32; o >= 1; o >>= 1)
#pragma unroll
    for (int r = 0; r < 2; ++r) s[r] += __shfl_xor(s[r], o);
#pragma unroll
  for (int r = 0; r < 2; ++r) {
    s[r] *= (1.0f / 512.0f);
    q[r] = 0.f;
#pragma unroll
    for (int j = 0; j < 8; ++j) { float dlt = x[r][j] - s[r]; q[r] += dlt * dlt; }
  }
#pragma unroll
  for (int o = 32; o >= 1; o >>= 1)
#pragma unroll
    for (int r = 0; r < 2; ++r) q[r] += __shfl_xor(q[r], o);
#pragma unroll
  for (int r = 0; r < 2; ++r) {
    const float m = rbf(s[r]);
    const float v = rbf(q[r] * (1.0f / 512.0f));
    const float den = rbf(sqrtf(rbf(v + 1e-5f)));
#pragma unroll
    for (int j = 0; j < 8; ++j) x[r][j] = rbf(rbf(x[r][j] - m) / den);
  }
}

__global__ __launch_bounds__(256) void kA(const uint16_t* __restrict__ x, const uint16_t* __restrict__ logA,
                                          const uint16_t* __restrict__ Aim, const uint16_t* __restrict__ Cre,
                                          const uint16_t* __restrict__ Cim, const uint16_t* __restrict__ logdt,
                                          uint16_t* __restrict__ xnT, uint16_t* __restrict__ hT,
                                          uint16_t* __restrict__ Khi, uint16_t* __restrict__ Klo) {
  __shared__ __attribute__((aligned(16))) char smem[67584];
  const int tid = threadIdx.x, lane = tid & 63, w = tid >> 6;
  if (blockIdx.x < 256) {
    uint16_t* txn = (uint16_t*)smem;
    uint16_t* th = (uint16_t*)(smem + 8320);
    const int s0 = ((blockIdx.x & 7) * 32 + (blockIdx.x >> 3)) * 8;
    float f[2][8];
    const int r0 = w * 2;
#pragma unroll
    for (int rr = 0; rr < 2; ++rr) {
      s16x8 v = *(const s16x8*)(x + (size_t)(s0 + r0 + rr) * D_ + lane * 8);
#pragma unroll
      for (int j = 0; j < 8; ++j) f[rr][j] = bf2f((uint16_t)v[j]);
    }
    ln_rows(f);
#pragma unroll
    for (int rr = 0; rr < 2; ++rr) {
      s16x8 o;
#pragma unroll
      for (int j = 0; j < 8; ++j) o[j] = (short)f2bf(f[rr][j]);
      *(s16x8*)(txn + (r0 + rr) * 520 + lane * 8) = o;
    }
    ln_rows(f);
#pragma unroll
    for (int rr = 0; rr < 2; ++rr) {
      s16x8 o;
#pragma unroll
      for (int j = 0; j < 8; ++j) o[j] = (short)f2bf(f[rr][j]);
      *(s16x8*)(th + (r0 + rr) * 520 + lane * 8) = o;
    }
    __syncthreads();
#pragma unroll
    for (int dd = 0; dd < 2; ++dd) {
      const int d = tid + dd * 256;
      s16x8 a, b;
#pragma unroll
      for (int r = 0; r < 8; ++r) {
        a[r] = (short)txn[r * 520 + d];
        b[r] = (short)th[r * 520 + d];
      }
      *(s16x8*)(xnT + (size_t)d * LT + s0) = a;
      *(s16x8*)(hT + (size_t)d * LT + s0) = b;
    }
    return;
  }
  const int d = blockIdx.x - 256;
  const float dt = rbf(__expf(bf2f(logdt[d])));
  float* XA = (float*)(smem + w * 16896);
  float* YB = (float*)(smem + w * 16896 + 8448);
  float* Kp = (float*)smem;
  const int pbase = (w == 0) ? 0 : (128 << w);
  const int hstep = (w == 0) ? 16 : (16 << w);
  const int lstep = 1 << w;
  const int nhi = (w == 0) ? 16 : 8;
  f32x4 acc = {0.f, 0.f, 0.f, 0.f}, acc2 = {0.f, 0.f, 0.f, 0.f};
  float k2048 = 0.f;
  for (int half = 0; half < 2; ++half) {
    const int n = half * 64 + lane;
    const float ar = -rbf(expf(bf2f(logA[d * N_ + n]))) * dt;
    const float ai = bf2f(Aim[d * N_ + n]) * dt;
    const float cr = bf2f(Cre[d * N_ + n]) * dt;
    const float ci = bf2f(Cim[d * N_ + n]) * dt;
    {
      float sn, cs, e;
      e = __expf(ar * (float)pbase); fsincos(ai * (float)pbase, &sn, &cs);
      float zr = e * cs, zi = e * sn;
      float xr = cr * zr - ci * zi, xi = cr * zi + ci * zr;
      e = __expf(ar * (float)hstep); fsincos(ai * (float)hstep, &sn, &cs);
      const float hr = e * cs, hi = e * sn;
      e = __expf(ar * (float)lstep); fsincos(ai * (float)lstep, &sn, &cs);
      const float lr = e * cs, li = e * sn;
      float yr = 1.f, yi = 0.f;
#pragma unroll
      for (int i = 0; i < 16; ++i) {
        XA[i * 132 + 2 * lane] = (i < nhi) ? xr : 0.f;
        XA[i * 132 + 2 * lane + 1] = (i < nhi) ? -xi : 0.f;
        YB[i * 132 + 2 * lane] = yr;
        YB[i * 132 + 2 * lane + 1] = yi;
        const float nxr = xr * hr - xi * hi, nxi = xr * hi + xi * hr;
        xr = nxr; xi = nxi;
        const float nyr = yr * lr - yi * li, nyi = yr * li + yi * lr;
        yr = nyr; yi = nyi;
      }
    }
    if (w == 3) {
      const float e = __expf(ar * 2048.f);
      float sn, cs;
      fsincos(ai * 2048.f, &sn, &cs);
      k2048 += e * (cr * cs - ci * sn);
    }
    __builtin_amdgcn_s_waitcnt(0);
    __builtin_amdgcn_wave_barrier();
#pragma unroll
    for (int ks = 0; ks < 32; ++ks) {
      const float a = XA[(lane & 15) * 132 + 4 * ks + (lane >> 4)];
      const float b = YB[(lane & 15) * 132 + 4 * ks + (lane >> 4)];
      if (ks & 1) acc2 = __builtin_amdgcn_mfma_f32_16x16x4f32(a, b, acc2, 0, 0, 0);
      else acc = __builtin_amdgcn_mfma_f32_16x16x4f32(a, b, acc, 0, 0, 0);
    }
    __builtin_amdgcn_s_waitcnt(0);
    __builtin_amdgcn_wave_barrier();
  }
  (void)Kp;
  __syncthreads();
  float* Kt = (float*)smem;
  {
    const int j = lane & 15;
    const int off = (w == 0) ? 0 : (128 + 128 * w);
#pragma unroll
    for (int r = 0; r < 4; ++r) {
      const int i = (lane >> 4) * 4 + r;
      if (i < nhi) Kt[off + i * 16 + j] = 2.f * (acc[r] + acc2[r]);
    }
    if (w == 3) {
      float t = wave_sum(k2048);
      if (lane == 0) Kt[640] = 2.f * t;
    }
  }
  __syncthreads();
  {
    s16x8 vh, vl;
#pragma unroll
    for (int j = 0; j < 8; ++j) {
      const int l = tid * 8 + j;
      const int p = (int)rbf((float)l);
      int idx;
      if (p < 256) idx = p;
      else if (p < 512) idx = 256 + ((p - 256) >> 1);
      else if (p < 1024) idx = 384 + ((p - 512) >> 2);
      else if (p < 2048) idx = 512 + ((p - 1024) >> 3);
      else idx = 640;
      const float kv = Kt[idx];
      const uint16_t h = f2bf(kv);
      vh[j] = (short)h;
      vl[j] = (short)f2bf(kv - bf2f(h));
    }
    *(s16x8*)(Khi + (size_t)d * L_ + tid * 8) = vh;
    *(s16x8*)(Klo + (size_t)d * L_ + tid * 8) = vl;
  }
}

__device__ __forceinline__ void glds16(const void* g, void* lds) {
  __builtin_amdgcn_global_load_lds((const __attribute__((address_space(1))) void*)g,
                                   (__attribute__((address_space(3))) void*)lds, 16, 0, 0);
}

template <bool SWAP>
__device__ __forceinline__ f32x16 gemm_tile(const uint16_t* __restrict__ P, int ldp, const uint16_t* __restrict__ Q,
                                            int ldq, int p0, int q0, int Kdim, char* smem) {
  const int tid = threadIdx.x, lane = tid & 63, w = tid >> 6;
  const int wp = w >> 1, wq = w & 1;
  f32x16 acc, acc2;
#pragma unroll
  for (int i = 0; i < 16; ++i) { acc[i] = 0.f; acc2[i] = 0.f; }
  const int nit = Kdim / 64;
  const int lrow = lane >> 3, lch = lane & 7;
  auto stage = [&](int it, int buf) {
    char* sp = smem + buf * 16384;
    char* sq = sp + 8192;
    const int k0 = it * 64;
#pragma unroll
    for (int t = 0; t < 2; ++t) {
      const int row = (w * 2 + t) * 8 + lrow;
      const int gch = lch ^ (row & 7);
      glds16(P + (size_t)(p0 + row) * ldp + k0 + gch * 8, sp + (w * 2 + t) * 1024);
      const int gchq = lch ^ (((row >> 1) & 1) << 2);
      glds16(Q + (size_t)(k0 + row) * ldq + q0 + gchq * 8, sq + (w * 2 + t) * 1024);
    }
  };
  stage(0, 0);
  stage(1, 1);
  stage(2, 2);
  for (int it = 0; it < nit; ++it) {
    if (it + 3 <= nit) asm volatile("s_waitcnt vmcnt(8)" ::: "memory");
    else asm volatile("s_waitcnt vmcnt(0)" ::: "memory");
    asm volatile("s_waitcnt lgkmcnt(0)" ::: "memory");
    __builtin_amdgcn_s_barrier();
    asm volatile("" ::: "memory");
    if (it + 3 < nit) stage(it + 3, (it + 3) & 3);
    const char* sp = smem + (it & 3) * 16384;
    const char* sq = sp + 8192;
    s16x8 pf[4], qf[4];
#pragma unroll
    for (int kk = 0; kk < 4; ++kk) {
      const int prow = wp * 32 + (lane & 31);
      const int pch = (2 * kk + (lane >> 5)) ^ (prow & 7);
      pf[kk] = *(const s16x8*)(sp + prow * 128 + pch * 16);
      s16x4 q4[2];
#pragma unroll
      for (int t = 0; t < 2; ++t) {
        const int krow = 16 * kk + 8 * (lane >> 5) + 4 * t + ((lane & 15) >> 2);
        const int col = wq * 32 + 16 * ((lane >> 4) & 1) + 4 * (lane & 3);
        const int ch = (col >> 3) ^ (((krow >> 1) & 1) << 2);
        q4[t] = __builtin_amdgcn_ds_read_tr16_b64_v4i16(
            (__attribute__((address_space(3))) s16x4*)(sq + krow * 128 + ch * 16 + (col & 7) * 2));
      }
      qf[kk] = s16x8{q4[0][0], q4[0][1], q4[0][2], q4[0][3], q4[1][0], q4[1][1], q4[1][2], q4[1][3]};
    }
    __builtin_amdgcn_sched_barrier(0);
#pragma unroll
    for (int kk = 0; kk < 4; ++kk) {
      if (kk & 1) {
        if (SWAP) acc2 = __builtin_amdgcn_mfma_f32_32x32x16_bf16(qf[kk], pf[kk], acc2, 0, 0, 0);
        else acc2 = __builtin_amdgcn_mfma_f32_32x32x16_bf16(pf[kk], qf[kk], acc2, 0, 0, 0);
      } else {
        if (SWAP) acc = __builtin_amdgcn_mfma_f32_32x32x16_bf16(qf[kk], pf[kk], acc, 0, 0, 0);
        else acc = __builtin_amdgcn_mfma_f32_32x32x16_bf16(pf[kk], qf[kk], acc, 0, 0, 0);
      }
    }
  }
#pragma unroll
  for (int i = 0; i < 16; ++i) acc[i] += acc2[i];
  return acc;
}

constexpr int CP_STRIDE = 4256;
constexpr int T0 = 2055;

__global__ __launch_bounds__(256) void kB(const uint16_t* __restrict__ hT, const uint16_t* __restrict__ w1,
                                          const uint16_t* __restrict__ b1, uint16_t* __restrict__ G,
                                          const uint16_t* __restrict__ xnT, const uint16_t* __restrict__ Khi,
                                          const uint16_t* __restrict__ Klo, const uint16_t* __restrict__ Dskip,
                                          uint16_t* __restrict__ AT) {
  __shared__ __attribute__((aligned(16))) char smem[65536];
  const int tid = threadIdx.x, lane = tid & 63, w = tid >> 6;
  if (blockIdx.x < 224) {
    const int xcd = blockIdx.x & 7, slot = blockIdx.x >> 3;
    const int id = xcd * 28 + slot;
    const int nt = id >> 3, mt = id & 7;
    const int p0 = mt * 64, q0 = nt * 64;
    f32x16 acc = gemm_tile<false>(hT, LT, w1, F_, p0, q0, L_, smem);
    const int wp = w >> 1, wq = w & 1;
    const int f = q0 + wq * 32 + (lane & 31);
    const float bias = bf2f(b1[f]);
#pragma unroll
    for (int r = 0; r < 16; ++r) {
      const int dd = p0 + wp * 32 + (r & 3) + 8 * (r >> 2) + 4 * (lane >> 5);
      const float u = rbf(rbf(acc[r]) + bias);
      const float inner = 0.7978845608028654f * (u + 0.044715f * u * u * u);
      const float g = 0.5f * u * (1.f + tanhf(inner));
      G[(size_t)dd * F_ + f] = f2bf(g);
    }
    return;
  }
  const int d = blockIdx.x - 224;
  uint16_t* cp = (uint16_t*)smem;
  uint16_t* Kz = (uint16_t*)(smem + 8 * CP_STRIDE);
  {
    for (int i = tid; i < 8 * CP_STRIDE / 4; i += 256) ((uint32_t*)smem)[i] = 0u;
    for (int i = tid; i < 2304 / 2; i += 256) {
      ((uint32_t*)Kz)[i] = 0u;
      ((uint32_t*)(Kz + 4608))[i] = 0u;
    }
    __syncthreads();
    s16x8 uv = *(const s16x8*)(xnT + (size_t)d * LT + tid * 8);
#pragma unroll
    for (int j = 0; j < 8; ++j) {
      const int l = tid * 8 + j;
      const int t = T0 - l;
#pragma unroll
      for (int c = 0; c < 8; ++c) cp[c * (CP_STRIDE / 2) + (t - c)] = (uint16_t)uv[j];
    }
    const int idx = 2048 + tid * 8;
    const int pos = idx + 8 * (idx >> 6);
    *(s16x8*)(Kz + pos) = *(const s16x8*)(Khi + (size_t)d * L_ + tid * 8);
    *(s16x8*)(Kz + 4608 + pos) = *(const s16x8*)(Klo + (size_t)d * L_ + tid * 8);
    __syncthreads();
  }
  f32x16 acc0, acc1;
#pragma unroll
  for (int i = 0; i < 16; ++i) { acc0[i] = 0.f; acc1[i] = 0.f; }
  const int r = lane & 31, h = lane >> 5;
  const int c = (T0 - r) & 7;
  const uint16_t* cpl = cp + c * (CP_STRIDE / 2);
  auto afrag = [&](int s) -> s16x8 {
    const int idx0 = T0 - (s + r - 8 * h) - c;
    return *(const s16x8*)(cpl + idx0);
  };
  const int tau0 = -3 + w * 32;
  s16x8 fa = afrag(16 * tau0);
  s16x8 fb = afrag(16 * (tau0 + 1));
  for (int st = 0; st < 32; ++st) {
    const int tau = tau0 + st;
    s16x8 fc = afrag(16 * (tau + 2));
    const int kidx = 2048 + 64 * r - 16 * tau + 8 * h;
    const int kpos = kidx + 8 * (kidx >> 6);
    s16x8 kh = *(const s16x8*)(Kz + kpos);
    s16x8 kl = *(const s16x8*)(Kz + 4608 + kpos);
    acc0 = __builtin_amdgcn_mfma_f32_32x32x16_bf16(fa, kh, acc0, 0, 0, 0);
    acc1 = __builtin_amdgcn_mfma_f32_32x32x16_bf16(fc, kh, acc1, 0, 0, 0);
    acc0 = __builtin_amdgcn_mfma_f32_32x32x16_bf16(fa, kl, acc0, 0, 0, 0);
    acc1 = __builtin_amdgcn_mfma_f32_32x32x16_bf16(fc, kl, acc1, 0, 0, 0);
    fa = fb;
    fb = fc;
  }
  __syncthreads();
  float* red = (float*)smem;
#pragma unroll
  for (int i = 0; i < 16; ++i) {
    red[((w * 2 + 0) * 16 + i) * 64 + lane] = acc0[i];
    red[((w * 2 + 1) * 16 + i) * 64 + lane] = acc1[i];
  }
  __syncthreads();
  {
    const int a = w >> 1;
    const float dsk = bf2f(Dskip[d]);
#pragma unroll
    for (int gg = 0; gg < 2; ++gg) {
      const int g = (w & 1) * 2 + gg;
      const int l0 = 64 * r + 32 * a + 8 * g + 4 * h;
      s16x4 un = *(const s16x4*)(xnT + (size_t)d * LT + l0);
      s16x4 o;
#pragma unroll
      for (int e = 0; e < 4; ++e) {
        const int reg = g * 4 + e;
        float y = 0.f;
#pragma unroll
        for (int ww = 0; ww < 4; ++ww) y += red[((ww * 2 + a) * 16 + reg) * 64 + lane];
        o[e] = (short)f2bf(rbf(y) + rbf(bf2f((uint16_t)un[e]) * dsk));
      }
      *(s16x4*)(AT + (size_t)d * LT + l0) = o;
    }
  }
}

__global__ __launch_bounds__(256) void kC(const uint16_t* __restrict__ G, const uint16_t* __restrict__ w2,
                                          const uint16_t* __restrict__ b2, const uint16_t* __restrict__ x,
                                          const uint16_t* __restrict__ AT, uint16_t* __restrict__ out) {
  __shared__ __attribute__((aligned(16))) char smem[65536];
  const int tid = threadIdx.x, lane = tid & 63, w = tid >> 6;
  const int xcd = blockIdx.x & 7, slot = blockIdx.x >> 3;
  const int id = xcd * 32 + slot;
  const int nt = id >> 3, mt = id & 7;
  const int p0 = mt * 64, q0 = nt * 64;
  f32x16 acc = gemm_tile<true>(G, F_, w2, L_, p0, q0, F_, smem);
  const int wp = w >> 1, wq = w & 1;
  const int dd = p0 + wp * 32 + (lane & 31);
#pragma unroll
  for (int g = 0; g < 4; ++g) {
    const int s0 = q0 + wq * 32 + 8 * g + 4 * (lane >> 5);
    s16x4 av = *(const s16x4*)(AT + (size_t)dd * LT + s0);
#pragma unroll
    for (int e = 0; e < 4; ++e) {
      const int s = s0 + e;
      const float bref = rbf(rbf(acc[g * 4 + e]) + bf2f(b2[s]));
      const float xa = rbf(bf2f(x[(size_t)s * D_ + dd]) + bf2f((uint16_t)av[e]));
      out[(size_t)s * D_ + dd] = f2bf(xa + bref);
    }
  }
}

extern "C" void kernel_launch(void* const* d_in, const int* in_sizes, int n_in, void* d_out, int out_size,
                              void* d_ws, size_t ws_size, hipStream_t stream) {
  const uint16_t* x = (const uint16_t*)d_in[0];
  const uint16_t* logA = (const uint16_t*)d_in[1];
  const uint16_t* Aim = (const uint16_t*)d_in[2];
  const uint16_t* Cre = (const uint16_t*)d_in[3];
  const uint16_t* Cim = (const uint16_t*)d_in[4];
  const uint16_t* logdt = (const uint16_t*)d_in[5];
  const uint16_t* Dskip = (const uint16_t*)d_in[6];
  const uint16_t* w1 = (const uint16_t*)d_in[8];
  const uint16_t* b1 = (const uint16_t*)d_in[9];
  const uint16_t* w2 = (const uint16_t*)d_in[10];
  const uint16_t* b2 = (const uint16_t*)d_in[11];
  char* ws = (char*)d_ws;
  const size_t MB2 = (size_t)3 << 20;
  uint16_t* xnT = (uint16_t*)(ws + 0 * MB2);
  uint16_t* hT = (uint16_t*)(ws + 1 * MB2);
  uint16_t* Khi = (uint16_t*)(ws + 2 * MB2);
  uint16_t* Klo = (uint16_t*)(ws + 3 * MB2);
  uint16_t* G = (uint16_t*)(ws + 4 * MB2);
  uint16_t* AT = (uint16_t*)(ws + 5 * MB2);
  kA<<<256 + 512, 256, 0, stream>>>(x, logA, Aim, Cre, Cim, logdt, xnT, hT, Khi, Klo);
  kB<<<224 + 512, 256, 0, stream>>>(hT, w1, b1, G, xnT, Khi, Klo, Dskip, AT);
  kC<<<256, 256, 0, stream>>>(G, w2, b2, x, AT, (uint16_t*)d_out);
}
```
